```python
import torch

def _softmax(x, axis=-1):
    return torch.softmax(x, dim=axis)

def _gelu(x):
    return torch.nn.functional.gelu(x, approximate='tanh')

def _silu(x):
    return torch.nn.functional.silu(x)

def _sigmoid(x):
    return torch.sigmoid(x)

def _layernorm(x, axis=-1, eps=1e-05):
    m = x.mean(dim=axis, keepdim=True)
    v = x.var(dim=axis, keepdim=True, unbiased=False)
    return (x - m) / torch.sqrt(v + eps)

def _scatter_add(tgt, idx, src, axis=0):
    return tgt.clone().index_add_(axis, idx, src)

def _segment_sum(src, seg, n):
    out = torch.zeros((n,) + src.shape[1:], dtype=src.dtype)
    return out.index_add_(0, seg, src)

def _pad(x, pw):
    flat = tuple((v for pair in reversed(pw) for v in pair))
    return torch.nn.functional.pad(x, flat)
kernel_name = 'gated_mix__memory_aug_recurrent_mem_tr__softmax_full_mha__prompt'

def setup_inputs():

    _w = lambda a: (lambda t: t.to(torch.bfloat16) if t.is_floating_point() else t)(torch.as_tensor(a))

    def _A_setup():
        B, S, Dm = (1, 2048, 512)
        H, Dh = (32, 128)
        M, seg = (8, 32)
        seg = min(seg, S)
        S = S // seg * seg
        return {'x': _w((lambda *s: torch.randn(*s, dtype=torch.bfloat16) * 0.05)(B, S, Dm)), 'mem0': _w((lambda *s: torch.randn(*s, dtype=torch.bfloat16) * 0.8)(B, M, Dm)), 'Wq': _w((lambda *s: torch.randn(*s, dtype=torch.bfloat16) * 1.6)(Dm, H, Dh) * Dm ** (-0.5)), 'Wk': _w((lambda *s: torch.randn(*s, dtype=torch.bfloat16) * 1.6)(Dm, H, Dh) * Dm ** (-0.5)), 'Wv': _w((lambda *s: torch.randn(*s, dtype=torch.bfloat16) * 0.8)(Dm, H, Dh) * Dm ** (-0.5)), 'Wo': _w((lambda *s: torch.randn(*s, dtype=torch.bfloat16) * 0.8)(H, Dh, Dm) * (H * Dh) ** (-0.5)), 'seg_len': seg}
    _A_in = _A_setup()

    def _B_setup():
        B, S, D = (1, 2048, 512)
        return {'x': _w((lambda *s: torch.randn(*s, dtype=torch.bfloat16) * 0.05)(B, S, D)), 'wqkv': _w((lambda *s: torch.randn(*s, dtype=torch.bfloat16) * 0.8)(D, 3 * D)), 'wo': _w((lambda *s: torch.randn(*s, dtype=torch.bfloat16) * 0.05)(D, D))}
    _B_in = _B_setup()
    return {'A_x': _A_in['x'], 'A_mem0': _A_in['mem0'], 'A_Wq': _A_in['Wq'], 'A_Wk': _A_in['Wk'], 'A_Wv': _A_in['Wv'], 'A_Wo': _A_in['Wo'], 'A_seg_len': _A_in['seg_len'], 'B_x': _B_in['x'], 'B_wqkv': _B_in['wqkv'], 'B_wo': _B_in['wo'], 'gate_w': _w((lambda *s: torch.randn(*s, dtype=torch.bfloat16) * 0.05)(512) * 16.0)}

def reference(A_x, A_mem0, A_Wq, A_Wk, A_Wv, A_Wo, A_seg_len, B_x, B_wqkv, B_wo, gate_w):

    def _A_ref(x, mem0, Wq, Wk, Wv, Wo, seg_len):
        B, S, Dm = x.shape
        H, Dh = (Wq.shape[1], Wq.shape[2])
        M = mem0.shape[1]
        scale = Dh ** (-0.5)
        n_seg = S // seg_len
        L = M + seg_len
        idx = torch.arange(L)
        mask = (idx[:, None] < M) | (idx[:, None] >= idx[None, :])
        mem = mem0
        outs = []
        for s in range(n_seg):
            xs = x[:, s * seg_len:(s + 1) * seg_len, :].clone()
            h = torch.concatenate([mem, xs], axis=1)
            q = torch.einsum('btd,dhe->bhte', h, Wq)
            k = torch.einsum('btd,dhe->bhte', h, Wk)
            v = torch.einsum('btd,dhe->bhte', h, Wv)
            scores = torch.einsum('bhqe,bhke->bhqk', q, k) * scale
            scores = torch.where(mask[None, None], scores, -1000000000.0)
            p = _softmax(scores, axis=-1)
            o = torch.einsum('bhqk,bhke->bhqe', p, v)
            o = torch.einsum('bhte,hed->btd', o, Wo)
            mem = o[:, :M, :].clone()
            outs.append(o[:, M:, :])
        return torch.concatenate(outs, axis=1)

    def _B_ref(x, wqkv, wo):
        B, S, D = x.shape
        H = 32
        Dh = D // H
        qkv = (lambda x, *d: x.permute(*d))((x @ wqkv).reshape(B, S, 3, H, Dh), 2, 0, 3, 1, 4)
        q, k, v = (qkv[0], qkv[1], qkv[2])
        scale = Dh ** (-0.5)
        scores = torch.einsum('bhqd,bhkd->bhqk', q, k) * scale
        p = _softmax(scores, axis=-1)
        o = torch.einsum('bhqk,bhkd->bhqd', p, v)
        return (lambda x, *d: x.permute(*d))(o, 0, 2, 1, 3).reshape(B, S, D) @ wo
    x = A_x
    g = _sigmoid((x * gate_w).sum(-1, keepdims=True))
    a = _A_ref(x, A_mem0, A_Wq, A_Wk, A_Wv, A_Wo, A_seg_len)
    b = _B_ref(x, B_wqkv, B_wo)
    return g * a + (1 - g) * b
```

```cpp
#include <hip/hip_runtime.h>
#include <hip/hip_bf16.h>
#include <type_traits>

typedef unsigned u32;
typedef unsigned long long u64;
typedef u32 u32x2 __attribute__((ext_vector_type(2)));
typedef u32 u32x4 __attribute__((ext_vector_type(4)));
typedef u32 u32x8 __attribute__((ext_vector_type(8)));
typedef float f32x2 __attribute__((ext_vector_type(2)));
typedef float f32x4 __attribute__((ext_vector_type(4)));
typedef float f32x16 __attribute__((ext_vector_type(16)));
typedef __bf16 bf16x2 __attribute__((ext_vector_type(2)));
typedef __bf16 bf16x8 __attribute__((ext_vector_type(8)));
typedef short s16x4 __attribute__((ext_vector_type(4)));
#define DI __device__ __forceinline__
#define LAS __attribute__((address_space(3)))

constexpr u32 MB = 1u << 20;
constexpr u32 WS_FLAGS = 0;
constexpr u32 FLAG_BYTES = 8192;
constexpr u32 WS_STAMP = 64 * 1024;
constexpr u32 WS_EXCH = 2 * MB;
constexpr u32 WS_WVOF = 8 * MB;
constexpr u32 WS_UXF = 24 * MB;
constexpr u32 WS_KXF = 88 * MB;
constexpr u32 WS_QXF = 104 * MB;
constexpr u32 WS_PXX = 120 * MB;
constexpr u32 WS_PXM = 124 * MB;
constexpr u32 WS_UF = 128 * MB;
constexpr u32 WS_BQF = 144 * MB;
constexpr u32 WS_BKF = 146 * MB;
constexpr u32 WS_BVF = 148 * MB;
constexpr u32 WS_OF = 150 * MB;
constexpr u32 WS_TS = 160 * MB;
constexpr u32 WS_END = 152 * MB;
constexpr u32 F_XPROG = 0;
constexpr u32 F_VPROG = 128;
constexpr u32 F_PXPROG = 256;
constexpr u32 F_UPROG = 384;
constexpr u32 F_WVO = 640;
constexpr u32 F_B1 = 768;
constexpr u32 F_B2 = 896;
constexpr u32 F_FILL = 1152;
constexpr u32 F_TMO = 1280;
constexpr int AUX_SC1 = 16;
constexpr u32 SPIN_CAP = 1u << 18;
#define DLY_DOWN 6
#define TSLEEP 16
#define DLY_RANGE 1500
#define DLY_UP 40
#define DLYQ0 2600
#define DLYQ00 1600
#define DLYP0 1400
#define DLYM0 400

struct Params {
    const char* x; const char* mem0; const char* wq; const char* wk; const char* wv; const char* wo;
    const char* wqkv; const char* bwo; const char* gw;
    char* out; char* ws;
    u32 dq, dp, dm, up, down, par, dbg, dq0;
};

typedef __amdgpu_buffer_rsrc_t rsrc_t;
DI u32 rfl(u32 v) { return __builtin_amdgcn_readfirstlane(v); }
DI u32 cvtpk(float lo, float hi) { f32x2 v = {lo, hi}; bf16x2 b = __builtin_convertvector(v, bf16x2); return __builtin_bit_cast(u32, b); }
DI u32x4 ld128(rsrc_t r, u32 voff, u32 soff) { return __builtin_amdgcn_raw_buffer_load_b128(r, voff, soff, AUX_SC1); }
DI u32x2 ld64(rsrc_t r, u32 voff, u32 soff) { return __builtin_amdgcn_raw_buffer_load_b64(r, voff, soff, AUX_SC1); }
DI u32 ld32(rsrc_t r, u32 voff, u32 soff) { return __builtin_amdgcn_raw_buffer_load_b32(r, voff, soff, AUX_SC1); }
DI void st128(u32x4 v, rsrc_t r, u32 voff, u32 soff) { __builtin_amdgcn_raw_buffer_store_b128(v, r, voff, soff, AUX_SC1); }
DI void st64(u32x2 v, rsrc_t r, u32 voff, u32 soff) { __builtin_amdgcn_raw_buffer_store_b64(v, r, voff, soff, AUX_SC1); }
DI void st32(u32 v, rsrc_t r, u32 voff, u32 soff) { __builtin_amdgcn_raw_buffer_store_b32(v, r, voff, soff, AUX_SC1); }
DI void bar() { asm volatile("s_waitcnt lgkmcnt(0)\n\ts_barrier" ::: "memory"); }
template <int N> DI void wait_vm() { asm volatile("s_waitcnt vmcnt(%0)" :: "i"(N) : "memory"); }
DI void pin() { __builtin_amdgcn_sched_barrier(0); }
DI u32x4 lds128(u32 a) { return *(const LAS u32x4*)(size_t)a; }
DI void lds_st128(u32 a, u32x4 v) { *(LAS u32x4*)(size_t)a = v; }
DI void lds_st64(u32 a, u32x2 v) { *(LAS u32x2*)(size_t)a = v; }
DI u32x2 lds_tr(u32 a) { return __builtin_bit_cast(u32x2, __builtin_amdgcn_ds_read_tr16_b64_v4i16((LAS s16x4*)(size_t)a)); }
DI u32x4 cat(u32x2 a, u32x2 b) { return u32x4{a.x, a.y, b.x, b.y}; }
DI void dma16(u32 voff, const char* sbase, u32 ldsaddr) {
    u32 keep;
    asm volatile("s_mov_b32 %0, m0\n\ts_mov_b32 m0, %3\n\ts_nop 0\n\tglobal_load_lds_dwordx4 %1, %2\n\ts_mov_b32 m0, %0"
                 : "=&s"(keep) : "v"(voff), "s"(sbase), "s"(ldsaddr) : "memory");
}
DI u32 wait_flags(rsrc_t ws, u32 word, u32 n, u32 want, u32 lane) {
    const u32 off = (word + (lane < n ? lane : 0)) * 4;
    u32 v;
    for (u32 spins = 0;; ++spins) {
        asm volatile("" ::: "memory");
        v = ld32(ws, off, 0);
        if (__builtin_amdgcn_ballot_w64(v < want) == 0) break;
        if (spins > SPIN_CAP) { st32(1u, ws, F_TMO * 4, 0); break; }
        __builtin_amdgcn_s_sleep(2);
    }
    asm volatile("" ::: "memory");
    return __builtin_amdgcn_ballot_w64(v < want + 8) == 0 ? want + 8 : want;
}
#define STAMP(i)
#define CY(var)
#define CS(i)
#define CSV(i, val)
#define CSDUMP(base)
#define CSD()
#define ACC(i, a, b)
#define TS(slot)
#define TSV(slot, val)
#define TSDUMP()
template <int SL>
DI void wait_flags_slow(rsrc_t ws, u32 word, u32 n, u32 want, u32 lane) {
    const u32 off = (word + (lane < n ? lane : 0)) * 4;
    for (u32 spins = 0;; ++spins) {
        asm volatile("" ::: "memory");
        const u32 v = ld32(ws, off, 0);
        if (__builtin_amdgcn_ballot_w64(v < want) == 0) break;
        if (spins > SPIN_CAP) { st32(4u, ws, F_TMO * 4, 0); break; }
        __builtin_amdgcn_s_sleep(SL);
    }
    asm volatile("" ::: "memory");
}
DI u32 swz(u32 row) { return ((row & 3) << 2) | ((row >> 2) & 3); }
DI void stage_rows(const char* gbase, u32 stride, u32 rows, u32 ldsbase, u32 w, u32 lane) {
    for (u32 i = w; i < rows / 4; i += 4) {
        const u32 row = 4 * i + (lane >> 4);
        dma16(row * stride + (((lane & 15) ^ swz(row)) << 4), gbase, ldsbase + i * 1024);
    }
}
DI u32 tr_addr(u32 ldsbase, u32 row0, u32 col0, u32 lane) {
    const u32 q = (lane & 15) >> 2, p = lane & 3, row = row0 + q, ch = (col0 >> 3) + (p >> 1);
    return ldsbase + 256 * row + ((ch ^ swz(row)) << 4) + 8 * (p & 1);
}
#define MF32_WA0(acc, wa, b) asm volatile("v_mfma_f32_32x32x16_bf16 %0, %1, %2, 0" : "=&v"(acc) : "a"(wa), "v"(b))
#define MF32_WA(acc, wa, b) asm volatile("v_mfma_f32_32x32x16_bf16 %0, %1, %2, %0" : "+v"(acc) : "a"(wa), "v"(b))
#define MF32_WB0(acc, a, wb) asm volatile("v_mfma_f32_32x32x16_bf16 %0, %1, %2, 0" : "=&v"(acc) : "v"(a), "a"(wb))
#define MF32_WB(acc, a, wb) asm volatile("v_mfma_f32_32x32x16_bf16 %0, %1, %2, %0" : "+v"(acc) : "v"(a), "a"(wb))
#define PIN_A(x) asm volatile("" : "+a"(x))
DI u32x4 pack_lo(const f32x16& a) { return u32x4{cvtpk(a[0], a[1]), cvtpk(a[2], a[3]), cvtpk(a[4], a[5]), cvtpk(a[6], a[7])}; }
DI u32x4 pack_hi(const f32x16& a) { return u32x4{cvtpk(a[8], a[9]), cvtpk(a[10], a[11]), cvtpk(a[12], a[13]), cvtpk(a[14], a[15])}; }

DI void stage_x(const char* x, u32 seg, u32 slot, u32 w, u32 lane) {
    const char* src = x + (size_t)seg * 32768;
#pragma unroll
    for (int i = 0; i < 8; ++i) {
        const u32 r = 8 * w + i;
        dma16(r * 1024 + ((lane ^ (r & 15)) << 4), src, slot + r * 1024);
    }
}
DI u32 ring3(u32 s) { const u32 m = s % 3; return m == 0 ? 131072u : (m - 1) * 32768u; }

DI void role_x(const Params& p, rsrc_t ws, u32 h, u32 w, u32 lane) {
    u32x4 Wq[32], Wk[32];
    stage_x(p.x, 0, ring3(0), w, lane);
#pragma unroll
    for (int rd = 0; rd < 2; ++rd) {
        stage_rows(p.wq + (size_t)rd * 256 * 8192 + h * 256, 8192, 256, 0, w, lane);
        stage_rows(p.wk + (size_t)rd * 256 * 8192 + h * 256, 8192, 256, 65536, w, lane);
        wait_vm<0>(); bar();
        const u32 hh = lane >> 5, blk = (lane >> 4) & 1;
        const u32 a0 = tr_addr(0, 8 * hh, 32 * w + 16 * blk, lane), a1 = tr_addr(0, 8 * hh + 4, 32 * w + 16 * blk, lane);
#pragma unroll
        for (int k = 0; k < 16; ++k) {
            Wq[16 * rd + k] = cat(lds_tr(a0 + k * 4096), lds_tr(a1 + k * 4096)); PIN_A(Wq[16 * rd + k]);
            Wk[16 * rd + k] = cat(lds_tr(a0 + 65536 + k * 4096), lds_tr(a1 + 65536 + k * 4096)); PIN_A(Wk[16 * rd + k]);
        }
        bar();
    }
    stage_x(p.x, 1, ring3(1), w, lane);
    STAMP(1);
    const u32 r = lane & 31, hh = lane >> 5;
    const u32 xb = r * 1024 + ((hh ^ (r & 15)) << 4);
    const u32 kt = (r >> 3) & 1, krho = (r & 3) + 4 * (((r >> 2) & 1) + 2 * (r >> 4));
    const u32 koff = WS_KXF + h * 8192 + kt * 4096 + w * 1024 + (krho + 16 * hh) * 16;
    const u32 qoff = WS_QXF + h * 8192 + (r >> 4) * 4096 + w * 1024 + ((r & 15) + 16 * hh) * 16;
    for (u32 s = 0; s < 64; ++s) {
        CY(c0);
        wait_vm<13>(); bar();
        CY(c1);
        stage_x(p.x, s + 2 < 64 ? s + 2 : 63, ring3(s + 2), w, lane);
        CY(c2);
        const u32 slot = ring3(s);
        u32 xa[8];
#pragma unroll
        for (int k = 0; k < 8; ++k) xa[k] = (xb ^ (k << 5)) + slot;
        f32x16 aq, ak;
        u32x4 f[4];
#pragma unroll
        for (int k = 0; k < 3; ++k) f[k] = lds128(xa[k]);
#pragma unroll
        for (int k = 0; k < 32; ++k) {
            if (k + 3 < 32) f[(k + 3) & 3] = lds128(xa[(k + 3) & 7] + ((k + 3) >> 3) * 256);
            if (k == 0) { MF32_WA0(aq, Wq[0], f[0]); MF32_WA0(ak, Wk[0], f[0]); }
            else { MF32_WA(aq, Wq[k], f[k & 3]); MF32_WA(ak, Wk[k], f[k & 3]); }
            pin();
        }
        CY(c3);
        wait_vm<8>();
        CY(c4);
        if (lane == 0) st32(s, ws, (F_XPROG + h * 4 + w) * 4, 0);
        asm volatile("s_nop 7\n\ts_nop 3" : "+v"(aq), "+v"(ak));
        const u32 so = s * 262144;
        st128(pack_lo(ak), ws, koff, so); st128(pack_hi(ak), ws, koff + 512, so);
        st128(pack_lo(aq), ws, qoff, so); st128(pack_hi(aq), ws, qoff + 512, so);
        CY(c5);
        ACC(0, c0, c1); ACC(1, c1, c2); ACC(2, c2, c3); ACC(3, c3, c4); ACC(4, c4, c5);
    }
    STAMP(2);
    wait_vm<0>();
    if (lane == 0) st32(64u, ws, (F_XPROG + h * 4 + w) * 4, 0);
}

DI void role_v(const Params& p, rsrc_t ws, u32 h, u32 w, u32 lane) {
    u32x4 Wv[32], Wo[8][4];
    stage_x(p.x, 0, ring3(0), w, lane);
    const u32 hh = lane >> 5, blk = (lane >> 4) & 1;
#pragma unroll
    for (int rd = 0; rd < 2; ++rd) {
        stage_rows(p.wv + (size_t)rd * 256 * 8192 + h * 256, 8192, 256, 0, w, lane);
        wait_vm<0>(); bar();
        const u32 a0 = tr_addr(0, 8 * hh, 32 * w + 16 * blk, lane), a1 = tr_addr(0, 8 * hh + 4, 32 * w + 16 * blk, lane);
#pragma unroll
        for (int k = 0; k < 16; ++k) { Wv[16 * rd + k] = cat(lds_tr(a0 + k * 4096), lds_tr(a1 + k * 4096)); PIN_A(Wv[16 * rd + k]); }
        bar();
    }
#pragma unroll
    for (int pn = 0; pn < 4; ++pn) stage_rows(p.wo + (size_t)h * 131072 + pn * 256, 1024, 128, pn * 32768, w, lane);
    wait_vm<0>(); bar();
#pragma unroll
    for (int ct = 0; ct < 4; ++ct) {
        const u32 a0 = tr_addr(w * 32768, 4 * hh, 32 * ct + 16 * blk, lane), a1 = tr_addr(w * 32768, 4 * hh + 8, 32 * ct + 16 * blk, lane);
#pragma unroll
        for (int kk = 0; kk < 8; ++kk) { Wo[kk][ct] = cat(lds_tr(a0 + kk * 4096), lds_tr(a1 + kk * 4096)); PIN_A(Wo[kk][ct]); }
    }
    bar();
    stage_x(p.x, 1, ring3(1), w, lane);
    STAMP(1);
    const u32 r = lane & 31;
    const u32 xb = r * 1024 + ((hh ^ (r & 15)) << 4);
    const u32 EXB = 65536;
    const u32 uv = 16 * w + 2 * (r >> 4) + ((r >> 1) & 1), uc = 2 * ((r >> 2) & 3) + (r & 1);
    const u32 uoff = WS_UXF + uv * 16384 + (h >> 1) * 1024 + (uc + 8 * (h & 1) + 16 * hh) * 16;
    for (u32 it = 0; it <= 64; ++it) {
        if (it == 1) wait_vm<8>(); else wait_vm<16>();
        bar();
        stage_x(p.x, it + 2 < 64 ? it + 2 : 63, ring3(it + 2), w, lane);
        f32x16 a1;
        if (it < 64) {
            const u32 slot = ring3(it);
            u32 xa[8];
#pragma unroll
            for (int k = 0; k < 8; ++k) xa[k] = (xb ^ (k << 5)) + slot;
            u32x4 f[4];
#pragma unroll
            for (int k = 0; k < 3; ++k) f[k] = lds128(xa[k]);
#pragma unroll
            for (int k = 0; k < 32; ++k) {
                if (k + 3 < 32) f[(k + 3) & 3] = lds128(xa[(k + 3) & 7] + ((k + 3) >> 3) * 256);
                if (k == 0) MF32_WA0(a1, Wv[0], f[0]); else MF32_WA(a1, Wv[k], f[k & 3]);
                pin();
            }
        }
        f32x16 a2[4];
        if (it > 0) {
            const u32 eb = EXB + ((it - 1) & 1) * 8192 + lane * 16;
            u32x4 g[4];
#pragma unroll
            for (int k = 0; k < 3; ++k) g[k] = lds128(eb + k * 1024);
#pragma unroll
            for (int kk = 0; kk < 8; ++kk) {
                if (kk + 3 < 8) g[(kk + 3) & 3] = lds128(eb + (kk + 3) * 1024);
#pragma unroll
                for (int ct = 0; ct < 4; ++ct) { if (kk == 0) MF32_WB0(a2[ct], g[0], Wo[0][ct]); else MF32_WB(a2[ct], g[kk & 3], Wo[kk][ct]); }
                pin();
            }
        }
        if (it < 64) {
            asm volatile("s_nop 7\n\ts_nop 3" : "+v"(a1));
            const u32 eb = EXB + (it & 1) * 8192 + 2 * w * 1024 + lane * 16;
            lds_st128(eb, pack_lo(a1)); lds_st128(eb + 1024, pack_hi(a1));
        }
        wait_vm<8>();
        if (it > 1 && lane == 0) st32(it - 1, ws, (F_VPROG + w * 32 + h) * 4, 0);
        if (it > 0) {
            asm volatile("s_nop 7\n\ts_nop 3" : "+v"(a2[0]), "+v"(a2[1]), "+v"(a2[2]), "+v"(a2[3]));
            const u32 so = (it - 1) * 1048576;
#pragma unroll
            for (int ct = 0; ct < 4; ++ct) { st128(pack_lo(a2[ct]), ws, uoff, so + ct * 65536); st128(pack_hi(a2[ct]), ws, uoff + 512, so + ct * 65536); }
        }
    }
    STAMP(2);
    wait_vm<0>();
    if (lane == 0) st32(64u, ws, (F_VPROG + w * 32 + h) * 4, 0);
}

DI void wvo_unit(const Params& p, rsrc_t ws, u32 unit, u32 w, u32 lane) {
    const u32 h = unit >> 2, qd = unit & 3;
#pragma unroll
    for (int pn = 0; pn < 4; ++pn) stage_rows(p.wo + (size_t)h * 131072 + pn * 256, 1024, 128, pn * 32768, w, lane);
    const u32 r = lane & 31, hh = lane >> 5, blk = (lane >> 4) & 1;
    u32x4 A[4][8];
#pragma unroll
    for (int rt = 0; rt < 4; ++rt)
#pragma unroll
        for (int k = 0; k < 8; ++k) A[rt][k] = *(const u32x4*)(p.wv + (size_t)(128 * qd + 32 * rt + r) * 8192 + h * 256 + k * 32 + hh * 16);
    wait_vm<0>(); bar();
    const u32 uv = 16 * w + 2 * (r >> 4) + ((r >> 1) & 1), uc = 2 * ((r >> 2) & 3) + (r & 1);
    const u32 obase = WS_WVOF + uv * 262144 + (h >> 3) * 65536 + ((h & 7) >> 1) * 16384 + (uc + 8 * (h & 1) + 16 * hh) * 16;
#pragma unroll
    for (int ct = 0; ct < 4; ++ct) {
        const u32 a0 = tr_addr(w * 32768, 8 * hh, 32 * ct + 16 * blk, lane), a1 = tr_addr(w * 32768, 8 * hh + 4, 32 * ct + 16 * blk, lane);
        u32x4 B[8];
#pragma unroll
        for (int k = 0; k < 8; ++k) B[k] = cat(lds_tr(a0 + k * 4096), lds_tr(a1 + k * 4096));
#pragma unroll
        for (int rt = 0; rt < 4; ++rt) {
            f32x16 acc = {};
#pragma unroll
            for (int k = 0; k < 8; ++k) acc = __builtin_amdgcn_mfma_f32_32x32x16_bf16(__builtin_bit_cast(bf16x8, A[rt][k]), __builtin_bit_cast(bf16x8, B[k]), acc, 0, 0, 0);
            const u32x4 lo = pack_lo(acc), hi = pack_hi(acc);
            auto s0 = __builtin_amdgcn_permlane32_swap(lo.x, lo.z, false, false);
            auto s1 = __builtin_amdgcn_permlane32_swap(lo.y, lo.w, false, false);
            auto s2 = __builtin_amdgcn_permlane32_swap(hi.x, hi.z, false, false);
            auto s3 = __builtin_amdgcn_permlane32_swap(hi.y, hi.w, false, false);
            const u32 T = 4 * qd + rt;
            const u32 so = (T >> 1) * 2048 + (T & 1) * 1024 + ct * 4 * 262144;
            st128(u32x4{s0[0], s1[0], s0[1], s1[1]}, ws, obase, so);
            st128(u32x4{s2[0], s3[0], s2[1], s3[1]}, ws, obase + 512, so);
        }
    }
    wait_vm<0>(); bar();
    if (w == 0 && lane == 0) st32(1u, ws, (F_WVO + unit) * 4, 0);
    STAMP(2);
}

DI u32 exch_off(u32 chunk, u32 par) {
    const u32 b0 = WS_EXCH + (par & 1) * 8192, b1 = WS_EXCH + 2 * MB - 127 * 16384 + ((par >> 1) & 1) * 8192;
    return (chunk >= 127 ? b1 : b0) + chunk * 16384;
}
DI u32 mem_off(u32 s, u32 par) { return exch_off(s, par); }
DI u32 pm_off(u32 s, u32 j, u32 par) { return exch_off(64 + 3 * s + j, par); }
#define SMF(acc, a, wb, idx) asm volatile("v_smfmac_f32_16x16x64_bf16 %0, %1, %2, %3" : "+v"(acc) : "v"(a), "a"(wb), "v"(idx))
#define MF16_0(acc, a, b) asm volatile("v_mfma_f32_16x16x32_bf16 %0, %1, %2, 0" : "=&v"(acc) : "v"(a), "v"(b))
#define MF16(acc, a, b) asm volatile("v_mfma_f32_16x16x32_bf16 %0, %1, %2, %0" : "+v"(acc) : "v"(a), "v"(b))
DI u32 umax3(u32 a, u32 b, u32 c) { const u32 m = a > b ? a : b; return m > c ? m : c; }
DI u32 nosent(u32 v) { return v < 0xFFFFFFFEu ? v : 0xFFFFFFFEu; }
constexpr u32 SENT = 0xFFFFFFFFu;
constexpr u32 OOB = 0xFFFFFF00u;

DI u32 now() { u64 t; asm volatile("s_memtime %0\n\ts_waitcnt lgkmcnt(0)" : "=s"(t) :: "memory"); return (u32)t; }
DI void wait_until(u32 t) { while ((int)(now() - t) < 0) {} }
DI void adapt(u32& dly, u32 nominal, u32 up, u32 down, bool missed) {
    const u32 d = missed ? dly + up : dly - down;
    dly = d > nominal + DLY_RANGE ? nominal + DLY_RANGE : (d + DLY_RANGE < nominal ? (nominal > DLY_RANGE ? nominal - DLY_RANGE : 0) : d);
}
DI bool has_sent(u32x4 a, u32x4 b) { u32 m = umax3(a.x, a.y, a.z); m = umax3(m, a.w, b.x); m = umax3(m, b.y, b.z); m = m > b.w ? m : b.w; return __builtin_amdgcn_ballot_w64(m == SENT) != 0; }

template <int WV, class Hook>
DI u32 gather_project(rsrc_t ws, u32 moff, u32 lane, const u32x8 (&W)[4][8], f32x4 (&acc)[4], u32 idx, u32 gbuf, u32 target, u64 (&cs)[8], Hook hook) {
#pragma unroll
    for (int t = 0; t < 4; ++t) { acc[t] = f32x4{0.f, 0.f, 0.f, 0.f}; asm volatile("" : "+v"(acc[t])); }
    const u32 wa = gbuf + (2 * WV) * 1024 + lane * 16, ra = gbuf + lane * 16, va = lane * 16 + (2 * WV) * 1024;
    wait_until(target);
    CS(0);
    u32 spins = 0;
    u32x4 fa, fb;
#pragma clang loop unroll(disable)
    for (;;) {
        fa = ld128(ws, va, moff); pin();
        fb = ld128(ws, va + 1024, moff); pin();
        if (!has_sent(fa, fb)) break;
        if (++spins > SPIN_CAP) { st32(2u, ws, F_TMO * 4, 0); break; }
        asm volatile("" ::: "memory");
    }
    CS(1);
    lds_st128(wa, fa); lds_st128(wa + 1024, fb);
    asm volatile("s_nop 1" : "+v"(acc[0]), "+v"(acc[1]), "+v"(acc[2]), "+v"(acc[3]));
    pin();
#pragma unroll
    for (int t = 0; t < 4; ++t) SMF(acc[t], fa, W[t][2 * WV], idx);
    bar();
    CS(2);
    u32x4 g[6];
    g[0] = lds128(ra + ((2 * WV + 2) & 7) * 1024); g[1] = lds128(ra + ((2 * WV + 3) & 7) * 1024);
    pin();
#pragma unroll
    for (int t = 0; t < 4; ++t) { SMF(acc[t], fb, W[t][2 * WV + 1], idx); g[2 + t] = lds128(ra + ((2 * WV + 4 + t) & 7) * 1024); pin(); }
#pragma unroll
    for (int j = 0; j < 6; ++j)
#pragma unroll
        for (int t = 0; t < 4; ++t) { SMF(acc[t], g[j], W[t][(2 * WV + 2 + j) & 7], idx); if (4 * j + t < 12) hook(4 * j + t); pin(); }
    asm volatile("s_nop 7" : "+v"(acc[0]), "+v"(acc[1]), "+v"(acc[2]), "+v"(acc[3]));
    CS(3);
    return spins;
}
DI float asf(u32 x) { return __builtin_bit_cast(float, x); }
DI u32 asu(float x) { return __builtin_bit_cast(u32, x); }
DI float swap_add32(float a, float b) {
    auto r = __builtin_amdgcn_permlane32_swap(asu(a), asu(b), false, false);
    const u32 r0 = r[0], r1 = r[1];
    return asf(r0) + asf(r1);
}
DI float max3f(float a, float b, float c) { float r; asm("v_max3_f32 %0, %1, %2, %3" : "=v"(r) : "v"(a), "v"(b), "v"(c)); return r; }
DI float max2f(float a, float b) { float r; asm("v_max_f32_e32 %0, %1, %2" : "=v"(r) : "v"(a), "v"(b)); return r; }
DI float xmax16(float a) { auto r = __builtin_amdgcn_permlane16_swap(asu(a), asu(a), false, false); const u32 r0 = r[0], r1 = r[1]; return max2f(asf(r0), asf(r1)); }
DI float xmax32(float a) { auto r = __builtin_amdgcn_permlane32_swap(asu(a), asu(a), false, false); const u32 r0 = r[0], r1 = r[1]; return max2f(asf(r0), asf(r1)); }
DI float xsum16(float a) { auto r = __builtin_amdgcn_permlane16_swap(asu(a), asu(a), false, false); const u32 r0 = r[0], r1 = r[1]; return asf(r0) + asf(r1); }
DI float xsum32(float a) { auto r = __builtin_amdgcn_permlane32_swap(asu(a), asu(a), false, false); const u32 r0 = r[0], r1 = r[1]; return asf(r0) + asf(r1); }
constexpr float SC_A = 0.08838834764831845f * 1.4426950408889634f;
DI void softmax12(f32x4& mm, f32x4& x0, f32x4& x1) {
    float m = max3f(max3f(mm[0], mm[1], mm[2]), max3f(mm[3], x0[0], x0[1]), max3f(x0[2], x0[3], x1[0]));
    m = max3f(m, max3f(x1[1], x1[2], x1[3]), m);
    m = xmax32(xmax16(m));
    const float mc = m * SC_A;
    float l = 0.f;
#pragma unroll
    for (int r = 0; r < 4; ++r) {
        mm[r] = __builtin_amdgcn_exp2f(mm[r] * SC_A - mc); x0[r] = __builtin_amdgcn_exp2f(x0[r] * SC_A - mc); x1[r] = __builtin_amdgcn_exp2f(x1[r] * SC_A - mc);
        l += mm[r] + x0[r] + x1[r];
    }
    l = xsum32(xsum16(l));
    const float inv = __builtin_amdgcn_rcpf(l);
#pragma unroll
    for (int r = 0; r < 4; ++r) { mm[r] *= inv; x0[r] *= inv; x1[r] *= inv; }
}

template <int WV>
DI void qk_loop(const Params& p, rsrc_t ws, const u32x8 (&W)[4][8], u32 h, u32 lane) {
    constexpr u32 w = WV;
    const u32 par = p.par;
    const u32 g = lane >> 4, c16 = lane & 15;
    const u32 idx = (lane & 8) ? 0xEEEEu : 0x4444u;
    const u32 wimg = (32 * w + c16) * 32 + (((2 * (lane >> 5) + (g & 1)) ^ ((c16 >> 2) & 3)) << 3);
    const u32 q4 = c16 >> 2, p4 = lane & 3;
    const u32 e0 = 4 * (g & 1) + 16 * (g >> 1) + q4;
    const u32 rimg0 = e0 * 32 + ((p4 ^ ((e0 >> 2) & 3)) << 3), rimg1 = (e0 + 8) * 32 + ((p4 ^ (((e0 + 8) >> 2) & 3)) << 3);
    const u32 pxo = c16 < 8 ? (h & 15) * 512 + (g * 8 + c16) * 16 : OOB;
    const u32 pmo = (c16 < 8 && lane >= 32) ? h * 128 + c16 * 16 + (g & 1) * 8 : OOB;
    const float ninf = lane < 32 ? -INFINITY : 0.f;
    u32 xknown = 0;
    auto prefetch = [&](u32 s) {
        if (xknown < s + 1) xknown = wait_flags(ws, F_XPROG + h * 4, 4, s + 1, lane);
        const u32 so = s * 262144 + h * 8192, dst = 32768 + (s & 1) * 16384 + lane * 16;
        u32x4 t[16];
#pragma unroll
        for (int q = 0; q < 8; ++q) { t[q] = ld128(ws, WS_KXF + q * 1024 + lane * 16, so); t[8 + q] = ld128(ws, WS_QXF + q * 1024 + lane * 16, so); }
#pragma unroll
        for (int q = 0; q < 16; ++q) lds_st128(dst + q * 1024, t[q]);
    };
    if (w == 3) prefetch(0);
    STAMP(2);
    u32 dly = w == 0 ? p.dq0 : p.dq;
    const u32 nominal = dly;
    u32 ref = now() - dly;
#pragma clang loop unroll(disable)
    for (u32 s = 0; s < 64; ++s) {
        f32x4 acc[4];
        u32x4 kx[8], qx[4];
        u64 cs[8] = {0, 0, 0, 0, 0, 0, 0, 0};
        const u32 kb = 32768 + (s & 1) * 16384 + lane * 16;
        const u32 nsw = gather_project<WV>(ws, mem_off(s, par), lane, W, acc, idx, 8192 + (s & 1) * 8192, ref + dly, cs, [&](int j) {
            if (w < 3 && j < 8) kx[j] = lds128(kb + j * 1024);
            if ((w == 1 || w == 2) && j >= 8) qx[j - 8] = lds128(kb + 8192 + (w - 1) * 4096 + (j - 8) * 1024);
        });
        const u32 img = (s & 1) * 4096;
        {
            float lo[4], hi[4];
#pragma unroll
            for (int r = 0; r < 4; ++r) { lo[r] = swap_add32(acc[0][r], acc[1][r]); hi[r] = swap_add32(acc[2][r], acc[3][r]); }
            lds_st64(img + wimg, u32x2{cvtpk(lo[0], lo[1]), cvtpk(lo[2], lo[3])});
            lds_st64(img + wimg + 512, u32x2{cvtpk(hi[0], hi[1]), cvtpk(hi[2], hi[3])});
        }
        bar();
        CS(4);
        if (w != 0) ref = now();
        if (w < 3) {
            u32x4 F[4];
#pragma unroll
            for (int k = 0; k < 4; ++k) { const u32x2 a = lds_tr(img + rimg0 + k * 1024), b = lds_tr(img + rimg1 + k * 1024); F[k] = cat(a, b); pin(); }
            f32x4 mm, x0, x1;
            if (w == 0) {
#pragma unroll
                for (int k = 0; k < 4; ++k) {
                    if (k == 0) { MF16_0(x0, kx[0], F[0]); MF16_0(x1, kx[4], F[0]); MF16_0(mm, F[0], F[0]); }
                    else { MF16(x0, kx[k], F[k]); MF16(x1, kx[4 + k], F[k]); MF16(mm, F[k], F[k]); }
                }
                asm volatile("s_nop 7" : "+v"(mm), "+v"(x0), "+v"(x1));
                CS(5);
                mm += ninf;
                softmax12(mm, x0, x1);
                const u32x4 px = {nosent(cvtpk(x0[0], x0[1])), nosent(cvtpk(x0[2], x0[3])), nosent(cvtpk(x1[0], x1[1])), nosent(cvtpk(x1[2], x1[3]))};
                const u32x2 pm = {nosent(cvtpk(mm[0], mm[1])), nosent(cvtpk(mm[2], mm[3]))};
                st128(px, ws, pxo, pm_off(s, h >> 4, par));
                st64(pm, ws, pmo, pm_off(s, 2, par));
                CS(6);
                pin();
                ref = now();
            } else {
#pragma unroll
                for (int k = 0; k < 4; ++k) {
                    if (k == 0) { MF16_0(x0, kx[0], qx[0]); MF16_0(x1, kx[4], qx[0]); MF16_0(mm, F[0], qx[0]); }
                    else { MF16(x0, kx[k], qx[k]); MF16(x1, kx[4 + k], qx[k]); MF16(mm, F[k], qx[k]); }
                }
                asm volatile("s_nop 7" : "+v"(mm), "+v"(x0), "+v"(x1));
                mm += ninf;
                const int qi = 16 * (w - 1) + c16, k0 = 4 * (g & 1) + 16 * (g >> 1);
#pragma unroll
                for (int r = 0; r < 4; ++r) { if (k0 + r > qi) x0[r] = -INFINITY; if (k0 + r + 8 > qi) x1[r] = -INFINITY; }
                softmax12(mm, x0, x1);
                const u32x4 px = {cvtpk(x0[0], x0[1]), cvtpk(x0[2], x0[3]), cvtpk(x1[0], x1[1]), cvtpk(x1[2], x1[3])};
                const u32x2 pm = {cvtpk(mm[0], mm[1]), cvtpk(mm[2], mm[3])};
                const u32 u = (s * 32 + h) * 2 + (w - 1);
                st128(px, ws, WS_PXX + lane * 16, u * 1024);
                st64(pm, ws, lane >= 32 ? WS_PXM + c16 * 16 + (g & 1) * 8 : OOB, u * 256);
            }
        } else {
            if (lane == 0) st32(s, ws, (F_PXPROG + h) * 4, 0);
            if (s < 63) prefetch(s + 1);
        }
        if (s > 0) adapt(dly, nominal, p.up, p.down, nsw != 0);
        CSV(7, nsw | (dly << 8));
        CSDUMP(0);
    }
    STAMP(3);
    CSD();
    wait_vm<0>(); bar();
    if (w == 3 && lane == 0) st32(64u, ws, (F_PXPROG + h) * 4, 0);
}

DI void role_qk(const Params& p, rsrc_t ws, u32 h, u32 w, u32 lane) {
    u32x8 W[4][8];
    const u32 g = lane >> 4, c16 = lane & 15;
#pragma unroll
    for (int rd = 0; rd < 2; ++rd) {
        stage_rows(p.wq + (size_t)rd * 256 * 8192 + h * 256, 8192, 256, 0, w, lane);
        stage_rows(p.wk + (size_t)rd * 256 * 8192 + h * 256, 8192, 256, 65536, w, lane);
        wait_vm<0>(); bar();
#pragma unroll
        for (int t = 0; t < 4; ++t) {
            const u32 base = (t & 1) * 65536, col0 = 32 * w + 16 * (t >> 1);
            const u32 a0 = tr_addr(base, 8 * g, col0, lane), a1 = tr_addr(base, 8 * g + 4, col0, lane);
#pragma unroll
            for (int k = 0; k < 4; ++k) {
                const u32x2 p0 = lds_tr(a0 + k * 16384), p1 = lds_tr(a1 + k * 16384), p2 = lds_tr(a0 + k * 16384 + 8192), p3 = lds_tr(a1 + k * 16384 + 8192);
                W[t][4 * rd + k] = u32x8{p0.x, p0.y, p1.x, p1.y, p2.x, p2.y, p3.x, p3.y};
                PIN_A(W[t][4 * rd + k]);
            }
        }
        bar();
    }
    STAMP(1);
    wait_flags(ws, F_FILL, 32, 1, lane);
    if (w == 0) qk_loop<0>(p, ws, W, h, lane);
    else if (w == 1) qk_loop<1>(p, ws, W, h, lane);
    else if (w == 2) qk_loop<2>(p, ws, W, h, lane);
    else qk_loop<3>(p, ws, W, h, lane);
}

template <int WV>
DI void vo_loop(const Params& p, rsrc_t ws, const u32x8 (&W)[4][8], u32 v, u32 lane) {
    constexpr u32 w = WV;
    const u32 par = p.par;
    const u32 idx = (lane & 8) ? 0xEEEEu : 0x4444u;
    const u32 g = lane >> 4, c16 = lane & 15, i8 = lane & 7, ab = (lane >> 3) & 1;
    const u32 moff_lane = (v >> 3) * 1024 + (16 * ((v >> 1) & 3) + 8 * (v & 1)) * 16;
    const u32 mo = (lane < 32 && c16 < 8) ? moff_lane + i8 * 16 + g * 8 : OOB;
    u32 vknown = 0;
    auto prefetch = [&](u32 s) {
        if (vknown < s + 1) vknown = wait_flags(ws, F_VPROG + (v >> 4) * 32, 32, s + 1, lane);
        constexpr int N = WV == 1 ? 6 : 5;
        const u32 so = WS_UXF + s * 1048576 + v * 16384 + (w - 1) * 1024, dst = 32768 + (s & 1) * 16384 + (w - 1) * 1024 + lane * 16;
        u32x4 t[N];
#pragma unroll
        for (int q = 0; q < N; ++q) t[q] = ld128(ws, q * 3072 + lane * 16, so);
#pragma unroll
        for (int q = 0; q < N; ++q) lds_st128(dst + q * 3072, t[q]);
    };
    if (w != 0) prefetch(0);
    const u32 pxl = ab * 512 + (g * 8 + i8) * 16;
    const u32 pml = (2 * (g >> 1) + ab) * 128 + i8 * 16 + (g & 1) * 8;
    STAMP(2);
    u32 dlym = p.dm, dlyp = p.dp;
    u32 refm = now() - dlym;
#pragma clang loop unroll(disable)
    for (u32 s = 0; s < 64; ++s) {
        f32x4 acc[4];
        u64 cs[8] = {0, 0, 0, 0, 0, 0, 0, 0};
        const u32 nsw = gather_project<WV>(ws, mem_off(s, par), lane, W, acc, idx, 8192 + (s & 1) * 8192, refm + dlym, cs, [](int) {});
        float lo[4], hi[4];
#pragma unroll
        for (int r = 0; r < 4; ++r) { lo[r] = swap_add32(acc[0][r], acc[1][r]); hi[r] = swap_add32(acc[2][r], acc[3][r]); }
        const u32x4 ub = {cvtpk(lo[0], lo[1]), cvtpk(lo[2], lo[3]), cvtpk(hi[0], hi[1]), cvtpk(hi[2], hi[3])};
        const u32 ubl = (s & 1) * 4096;
        lds_st128(ubl + w * 1024 + lane * 16, ub);
        const u32 refp = now();
        bar();
        u32 spins = 0;
        if (w == 0) {
            if (s < 63) {
                u32x4 ubf[4], ux[16];
#pragma unroll
                for (int q = 0; q < 16; ++q) ux[q] = lds128(32768 + (s & 1) * 16384 + q * 1024 + lane * 16);
#pragma unroll
                for (int q = 0; q < 4; ++q) ubf[q] = lds128(ubl + q * 1024 + lane * 16);
                const u32 o0 = pm_off(s, 0, par), o1 = pm_off(s, 1, par), o2 = pm_off(s, 2, par);
                f32x4 a2, a3;
#pragma unroll
                for (int q = 0; q < 16; ++q) asm volatile("" : "+v"(ux[q]));
#pragma unroll
                for (int q = 0; q < 4; ++q) asm volatile("" : "+v"(ubf[q]));
                wait_until(refp + dlyp);
                CS(4);
                auto sweep = [&]() -> bool {
                    u32x4 px[16]; u32x2 pa[4], pb[4];
#pragma unroll
                    for (int q = 0; q < 16; ++q) { px[q] = ld128(ws, pxl + (q & 7) * 1024, q < 8 ? o0 : o1); pin(); }
#pragma unroll
                    for (int q = 0; q < 4; ++q) { pa[q] = ld64(ws, pml + q * 1024, o2); pb[q] = ld64(ws, pml + q * 1024 + 512, o2); pin(); }
                    u32 m = umax3(umax3(px[0].x, px[0].y, px[0].z), px[0].w, 0u);
                    if (__builtin_amdgcn_ballot_w64(m == SENT) != 0) return false;
#pragma unroll
                    for (int q = 0; q < 16; q += 2) {
                        if (q == 0) MF16_0(a2, ux[0], px[0]); else MF16(a2, ux[q], px[q]);
                        if (q > 0) { m = umax3(m, px[q].x, px[q].y); m = umax3(m, px[q].z, px[q].w); }
                        pin();
                        if (q == 0) MF16_0(a3, ux[1], px[1]); else MF16(a3, ux[q + 1], px[q + 1]);
                        m = umax3(m, px[q + 1].x, px[q + 1].y); m = umax3(m, px[q + 1].z, px[q + 1].w);
                        pin();
                    }
#pragma unroll
                    for (int q = 0; q < 4; ++q) {
                        const u32x4 pm = cat(pa[q], pb[q]);
                        if (q & 1) MF16(a3, ubf[q], pm); else MF16(a2, ubf[q], pm);
                        m = umax3(m, pm.x, pm.y); m = umax3(m, pm.z, pm.w);
                        pin();
                    }
                    asm volatile("s_nop 7" : "+v"(a2), "+v"(a3));
                    return __builtin_amdgcn_ballot_w64(m == SENT) == 0;
                };
#pragma clang loop unroll(disable)
                while (!sweep()) {
                    if (++spins > SPIN_CAP) { st32(3u, ws, F_TMO * 4, 0); break; }
                    asm volatile("" ::: "memory");
                }
                CS(5);
                float y[4];
#pragma unroll
                for (int r = 0; r < 4; ++r) {
                    const float ar = a2[r] + a3[r];
                    auto t = __builtin_amdgcn_permlane32_swap(asu(ar), asu(ar), false, false);
                    const u32 t0 = t[0], t1 = t[1];
                    const u32 up = __builtin_amdgcn_update_dpp(0u, t1, 0x108, 0xf, 0xf, false);
                    y[r] = asf(t0) + asf(up);
                }
                const u32x2 o = {nosent(cvtpk(y[0], y[1])), nosent(cvtpk(y[2], y[3]))};
                st64(o, ws, mo, mem_off(s + 1, par));
                CS(6);
                pin();
            }
        } else {
            if (w == 3 && lane == 0) st32(s, ws, (F_UPROG + v) * 4, 0);
            if (w == 1) st128(lds128(ubl + lane * 16), ws, WS_UF + lane * 16, (s * 64 + v) * 4096);
            st128(ub, ws, WS_UF + lane * 16, (s * 64 + v) * 4096 + w * 1024);
            if (s < 63) prefetch(s + 1);
        }
        bar();
        refm = now();
        if (s > 0) { adapt(dlym, p.dm, p.up, p.down, nsw != 0); adapt(dlyp, p.dp, p.up, p.down, spins != 0); }
        CSV(7, nsw | (spins << 4) | (dlyp << 8));
        CSDUMP(0);
    }
    CSD();
    wait_vm<0>(); bar();
    if (w == 3 && lane == 0) st32(64u, ws, (F_UPROG + v) * 4, 0);
}

DI void role_vo(const Params& p, rsrc_t ws, u32 v, u32 w, u32 lane) {
    u32x8 W[4][8];
    const u32 par = p.par;
    const u32 ks = v >> 3, ga = (v >> 1) & 3, hf = v & 1;
    const u32 moff_lane = ks * 1024 + (16 * ga + 8 * hf) * 16;
    wait_flags(ws, F_FILL, 32, 1, lane);
    if (w == 0) {
        const u32 i = lane >> 2, cp = lane & 3;
        u32 val = 0;
        if (lane < 32) val = *(const u32*)(p.mem0 + i * 1024 + (64 * ks + 16 * ga + 4 * cp + 2 * hf) * 2);
        st32(nosent(val), ws, lane < 32 ? moff_lane + i * 16 + cp * 4 : OOB, mem_off(0, par));
    }
    wait_flags(ws, F_WVO, 64, 1, lane); wait_flags(ws, F_WVO + 64, 64, 1, lane);
#pragma unroll
    for (int t = 0; t < 4; ++t)
#pragma unroll
        for (int k = 0; k < 8; ++k) {
            const u32 so = WS_WVOF + v * 262144 + w * 65536 + t * 16384 + k * 2048;
            const u32x4 a = ld128(ws, lane * 16, so), b = ld128(ws, lane * 16 + 1024, so);
            W[t][k] = u32x8{a.x, a.y, a.z, a.w, b.x, b.y, b.z, b.w};
            PIN_A(W[t][k]);
        }
    STAMP(1);
    if (w == 0) vo_loop<0>(p, ws, W, v, lane);
    else if (w == 1) vo_loop<1>(p, ws, W, v, lane);
    else if (w == 2) vo_loop<2>(p, ws, W, v, lane);
    else vo_loop<3>(p, ws, W, v, lane);
}

DI void fill_exch(rsrc_t ws, u32 k, u32 tid) {
    const u32x4 sv = {SENT, SENT, SENT, SENT};
    for (u32 i = 0; i < 32; ++i) st128(sv, ws, WS_EXCH + k * 131072 + i * 4096 + tid * 16, 0);
    wait_vm<0>(); bar();
    if (tid == 0) st32(1u, ws, (F_FILL + k) * 4, 0);
}

DI void b1_unit(const Params& p, rsrc_t ws, u32 unit, u32 w, u32 lane) {
    const u32 cg = unit >> 3, rg = unit & 7;
    u32x4 Wf[32];
    const u32 hh = lane >> 5, blk = (lane >> 4) & 1, r = lane & 31;
    stage_x(p.x, 8 * rg, ring3(0), w, lane);
#pragma unroll
    for (int rd = 0; rd < 2; ++rd) {
        stage_rows(p.wqkv + (size_t)rd * 256 * 3072 + cg * 256, 3072, 256, 0, w, lane);
        wait_vm<0>(); bar();
        const u32 a0 = tr_addr(0, 8 * hh, 32 * w + 16 * blk, lane), a1 = tr_addr(0, 8 * hh + 4, 32 * w + 16 * blk, lane);
#pragma unroll
        for (int k = 0; k < 16; ++k) { Wf[16 * rd + k] = cat(lds_tr(a0 + k * 4096), lds_tr(a1 + k * 4096)); PIN_A(Wf[16 * rd + k]); }
        bar();
    }
    const u32 xb = r * 1024 + ((hh ^ (r & 15)) << 4);
    const u32 h0 = 8 * (cg & 3) + 2 * w;
    const bool isv = cg >= 8;
    const u32 obase = isv ? WS_BVF + (h0 + blk) * 65536 + hh * 256 + (lane & 15) * 16 : (cg < 4 ? WS_BQF : WS_BKF) + h0 * 65536 + lane * 16;
    const u32 ohi = isv ? 512 : 65536;
    for (u32 i = 0; i < 8; ++i) {
        if (i + 1 < 8) stage_x(p.x, 8 * rg + i + 1, ring3(i + 1), w, lane);
        if (i + 1 < 8) wait_vm<8>(); else wait_vm<0>();
        bar();
        const u32 slot = ring3(i);
        f32x16 a;
        u32x4 f[4];
#pragma unroll
        for (int k = 0; k < 3; ++k) f[k] = lds128((xb ^ (k << 5)) + slot);
        if (!isv) {
#pragma unroll
            for (int k = 0; k < 32; ++k) {
                if (k + 3 < 32) f[(k + 3) & 3] = lds128((xb ^ (((k + 3) & 7) << 5)) + slot + ((k + 3) >> 3) * 256);
                if (k == 0) MF32_WA0(a, Wf[0], f[0]); else MF32_WA(a, Wf[k], f[k & 3]);
                pin();
            }
            asm volatile("s_nop 7\n\ts_nop 3" : "+v"(a));
            if (cg < 4) a *= 0.25f * 1.4426950408889634f;
        } else {
#pragma unroll
            for (int k = 0; k < 32; ++k) {
                if (k + 3 < 32) f[(k + 3) & 3] = lds128((xb ^ (((k + 3) & 7) << 5)) + slot + ((k + 3) >> 3) * 256);
                if (k == 0) MF32_WB0(a, f[0], Wf[0]); else MF32_WB(a, f[k & 3], Wf[k]);
                pin();
            }
            asm volatile("s_nop 7\n\ts_nop 3" : "+v"(a));
        }
        const u32 so = (8 * rg + i) * 1024;
        st128(pack_lo(a), ws, obase, so); st128(pack_hi(a), ws, obase + ohi, so);
    }
    wait_vm<0>(); bar();
    if (w == 0 && lane == 0) st32(1u, ws, (F_B1 + unit) * 4, 0);
}

#define MF32V0(acc, a, b) asm volatile("v_mfma_f32_32x32x16_bf16 %0, %1, %2, 0" : "=&v"(acc) : "v"(a), "v"(b))
#define MF32VC(acc, a, b, c) asm volatile("v_mfma_f32_32x32x16_bf16 %0, %1, %2, %3" : "=&v"(acc) : "v"(a), "v"(b), "v"(c))
#define MF32V(acc, a, b) asm volatile("v_mfma_f32_32x32x16_bf16 %0, %1, %2, %0" : "+v"(acc) : "v"(a), "v"(b))
constexpr float B2_THR = 16.f;
DI float max16(const f32x16& s) {
    return max3f(max3f(max3f(s[0], s[1], s[2]), max3f(s[3], s[4], s[5]), max3f(s[6], s[7], s[8])),
                 max3f(s[9], s[10], s[11]), max3f(max3f(s[12], s[13], s[14]), s[15], s[15]));
}
DI void role_b2(const Params& p, rsrc_t ws, u32 h, u32 half, u32 w, u32 lane) {
    const u32 VB = 65536;
    wait_flags(ws, F_B1 + (h >> 3) * 8, 8, 1, lane); wait_flags(ws, F_B1 + (4 + (h >> 3)) * 8, 8, 1, lane); wait_flags(ws, F_B1 + (8 + (h >> 3)) * 8, 8, 1, lane);
    {
        const u32 vdst = VB + (lane >> 4) * 272 + (lane & 15) * 16;
        for (u32 i = 0; i < 2; ++i) {
            u32x4 t[16];
#pragma unroll
            for (int q = 0; q < 8; ++q) { const u32 tile = w * 16 + i * 8 + q; t[q] = ld128(ws, WS_BKF + lane * 16, (h * 64 + tile) * 1024); t[8 + q] = ld128(ws, WS_BVF + lane * 16, (h * 64 + tile) * 1024); }
#pragma unroll
            for (int q = 0; q < 8; ++q) { const u32 tile = w * 16 + i * 8 + q; lds_st128(tile * 1024 + lane * 16, t[q]); lds_st128(vdst + tile * 1088, t[8 + q]); }
        }
        const u32x4 ones = {0x3F803F80u, 0x3F803F80u, 0x3F803F80u, 0x3F803F80u};
        lds_st128(VB + (w * 64 + lane) * 272 + 256, ones);
    }
    bar();
    const u32 hh = lane >> 5, r = lane & 31;
    const u32 vrd = VB + hh * 272 + (r < 16 ? r * 16 : 256);
    const u32 blk = half * 4 + w;
    for (u32 j = 0; j < 4; ++j) {
        const u32 T0 = 16 * j + 2 * blk;
        u32x4 qf[2];
        qf[0] = ld128(ws, WS_BQF + lane * 16, (h * 64 + T0) * 1024); qf[1] = ld128(ws, WS_BQF + lane * 16, (h * 64 + T0 + 1) * 1024);
        f32x16 O[2], S[2], NM[2];
        float m[2];
        u32x4 kf = lds128(lane * 16);
#pragma unroll
        for (int u = 0; u < 2; ++u) MF32V0(S[u], kf, qf[u]);
        asm volatile("s_nop 7\n\ts_nop 3" : "+v"(S[0]), "+v"(S[1]));
#pragma unroll
        for (int u = 0; u < 2; ++u) {
            m[u] = xmax32(max16(S[u]));
#pragma unroll
            for (int i = 0; i < 16; ++i) { S[u][i] -= m[u]; NM[u][i] = -m[u]; O[u][i] = 0.f; }
        }
        f32x16 S2[2];
        auto step = [&](u32 kt, f32x16 (&Sc)[2], f32x16 (&Sn)[2]) {
            const u32 kn = kt + 1 < 64 ? kt + 1 : 63;
            kf = lds128(kn * 1024 + lane * 16);
            const u32x4 v0 = lds128(vrd + kt * 1088), v1 = lds128(vrd + kt * 1088 + 544);
#pragma unroll
            for (int u = 0; u < 2; ++u) {
                MF32VC(Sn[u], kf, qf[u], NM[u]);
                pin();
                const float t = max16(Sc[u]);
                if (__builtin_amdgcn_ballot_w64(t > B2_THR) != 0) {
                    asm volatile("s_nop 7\n\ts_nop 7" : "+v"(Sn[u]), "+v"(O[u]), "+v"(NM[u]));
                    const float d = fmaxf(xmax32(t), 0.f), f = __builtin_amdgcn_exp2f(-d);
                    m[u] += d;
#pragma unroll
                    for (int i = 0; i < 16; ++i) { Sc[u][i] -= d; Sn[u][i] -= d; O[u][i] *= f; NM[u][i] = -m[u]; }
                    asm volatile("s_nop 1" : "+v"(Sn[u]), "+v"(O[u]), "+v"(NM[u]));
                }
                u32x4 p0, p1;
#pragma unroll
                for (int i = 0; i < 4; ++i) p0[i] = cvtpk(__builtin_amdgcn_exp2f(Sc[u][2 * i]), __builtin_amdgcn_exp2f(Sc[u][2 * i + 1]));
                asm volatile("s_nop 1" : "+v"(p0));
                MF32V(O[u], v0, p0);
                pin();
#pragma unroll
                for (int i = 0; i < 4; ++i) p1[i] = cvtpk(__builtin_amdgcn_exp2f(Sc[u][8 + 2 * i]), __builtin_amdgcn_exp2f(Sc[u][8 + 2 * i + 1]));
                asm volatile("s_nop 1" : "+v"(p1));
                MF32V(O[u], v1, p1);
                pin();
            }
        };
        asm volatile("s_nop 1" : "+v"(NM[0]), "+v"(NM[1]), "+v"(O[0]), "+v"(O[1]));
#pragma clang loop unroll(disable)
        for (u32 kt = 0; kt < 64; kt += 2) { step(kt, S, S2); step(kt + 1, S2, S); }
        asm volatile("s_nop 7\n\ts_nop 7" : "+v"(O[0]), "+v"(O[1]));
#pragma unroll
        for (int u = 0; u < 2; ++u) {
            const float inv = 1.0f / O[u][8];
            const u32x4 o = {cvtpk(O[u][0] * inv, O[u][1] * inv), cvtpk(O[u][2] * inv, O[u][3] * inv), cvtpk(O[u][4] * inv, O[u][5] * inv), cvtpk(O[u][6] * inv, O[u][7] * inv)};
            st128(o, ws, WS_OF + (r >> 4) * 16384 + (h >> 1) * 1024 + ((r & 15) + 16 * (2 * (h & 1) + hh)) * 16, (T0 + u) * 32768);
        }
        wait_vm<0>();
        if (lane == 0) st32(j + 1, ws, (F_B2 + blk * 32 + h) * 4, 0);
    }
}

#define MF16A0(acc, wa, b) asm volatile("v_mfma_f32_16x16x32_bf16 %0, %1, %2, 0" : "=&v"(acc) : "a"(wa), "v"(b))
#define MF16A(acc, wa, b) asm volatile("v_mfma_f32_16x16x32_bf16 %0, %1, %2, %0" : "+v"(acc) : "a"(wa), "v"(b))
DI void role_t(const Params& p, rsrc_t ws, u32 kb, u32 ph, u32 w, u32 lane) {
    const u32 g = lane >> 4, cc = lane & 15;
    u32x4 Wb[4][4], Gw[4];
    stage_rows(p.bwo + (kb >> 1) * 256, 1024, 512, 0, w, lane);
#pragma unroll
    for (int kk = 0; kk < 4; ++kk) { Gw[kk] = *(const u32x4*)(p.gw + (128 * w + 32 * kk + 8 * g) * 2); PIN_A(Gw[kk]); }
    wait_vm<0>(); bar();
#pragma unroll
    for (int kk = 0; kk < 4; ++kk)
#pragma unroll
        for (int j = 0; j < 4; ++j) {
            const u32 row0 = 128 * w + 32 * kk + 16 * (g >> 1) + 4 * (g & 1), col0 = 64 * (kb & 1) + 16 * j;
            Wb[kk][j] = cat(lds_tr(tr_addr(0, row0, col0, lane)), lds_tr(tr_addr(0, row0 + 8, col0, lane)));
            PIN_A(Wb[kk][j]);
        }
    bar();
    const u32 hf = (cc >> 1) & 1, c8 = 2 * (cc >> 2) + (cc & 1);
    const u32 uxo = WS_UXF + (8 * kb + hf) * 16384 + 4 * w * 1024 + (c8 + 16 * g) * 16;
    const u32 uo = WS_UF + (8 * kb + hf) * 4096 + w * 1024 + (c8 + 16 * g) * 16;
    const u32 pxo = WS_PXX + 8 * w * 2048 + lane * 16;
    const u32 pmo = WS_PXM + (8 * w + 2 * (g >> 1)) * 512 + cc * 16 + (g & 1) * 8;
    const u32 oo = WS_OF + 4 * w * 1024 + lane * 16;
    const u32 RA = 0, RB = 34816, RZ = 69632;
    if (p.dbg & 64) { for (int i = 0; i < 3000; ++i) __builtin_amdgcn_s_sleep(4); }
    for (u32 s = ph; s < 64; s += 4) {
        if (w == 0) {
            wait_flags_slow<TSLEEP>(ws, F_B2 + ((s >> 1) & 7) * 32, 32, (s >> 4) + 1, lane);
            wait_flags_slow<TSLEEP>(ws, F_VPROG + (kb >> 1) * 32, 32, s + 1, lane);
            wait_flags_slow<TSLEEP>(ws, F_PXPROG, 32, s + 1, lane);
            wait_flags_slow<TSLEEP>(ws, F_UPROG + 8 * kb, 8, s + 1, lane);
        }
        bar();
        f32x4 aa[2][4], ab_[2][4], az[2];
        u32x4 uB[2][4], pM[2][2], xs[4][2];
#pragma unroll
        for (int a = 0; a < 2; ++a) {
#pragma unroll
            for (int j = 0; j < 4; ++j) uB[a][j] = ld128(ws, uo + a * 128, s * 262144 + j * 8192);
#pragma unroll
            for (int qt = 0; qt < 2; ++qt) pM[a][qt] = cat(ld64(ws, pmo + a * 512 + qt * 256, s * 16384), ld64(ws, pmo + a * 512 + qt * 256 + 2048, s * 16384));
        }
#pragma unroll
        for (int kk = 0; kk < 4; ++kk)
#pragma unroll
            for (int qt = 0; qt < 2; ++qt) xs[kk][qt] = *(const u32x4*)(p.x + (size_t)(32 * s + 16 * qt + cc) * 1024 + (128 * w + 32 * kk + 8 * g) * 2);
        u32x4 oF[4][2];
#pragma unroll
        for (int kk = 0; kk < 4; ++kk)
#pragma unroll
            for (int qt = 0; qt < 2; ++qt) oF[kk][qt] = ld128(ws, oo + kk * 1024, s * 32768 + qt * 16384);
        u32x4 ux[2][2][4], px[2][2][2];
        auto load_pair = [&](int b, int hp) {
#pragma unroll
            for (int a = 0; a < 2; ++a) {
#pragma unroll
                for (int j = 0; j < 4; ++j) ux[b][a][j] = ld128(ws, uxo + hp * 1024 + a * 128, s * 1048576 + j * 32768);
#pragma unroll
                for (int qt = 0; qt < 2; ++qt) px[b][a][qt] = ld128(ws, pxo + (2 * hp + a) * 2048 + qt * 1024, s * 65536);
            }
        };
        load_pair(0, 0);
        pin();
#pragma unroll
        for (int qt = 0; qt < 2; ++qt) {
#pragma unroll
            for (int j = 0; j < 4; ++j) { MF16_0(aa[qt][j], uB[0][j], pM[0][qt]); MF16(aa[qt][j], uB[1][j], pM[1][qt]); }
#pragma unroll
            for (int kk = 0; kk < 4; ++kk) { if (kk == 0) MF16A0(az[qt], Gw[0], xs[0][qt]); else MF16A(az[qt], Gw[kk], xs[kk][qt]); }
        }
        pin();
        load_pair(1, 1);
        pin();
#pragma unroll
        for (int kk = 0; kk < 4; ++kk)
#pragma unroll
            for (int qt = 0; qt < 2; ++qt)
#pragma unroll
                for (int j = 0; j < 4; ++j) { if (kk == 0) MF16A0(ab_[qt][j], Wb[0][j], oF[0][qt]); else MF16A(ab_[qt][j], Wb[kk][j], oF[kk][qt]); }
        pin();
#pragma unroll
        for (int hp = 0; hp < 4; ++hp) {
#pragma unroll
            for (int a = 0; a < 2; ++a)
#pragma unroll
                for (int qt = 0; qt < 2; ++qt)
#pragma unroll
                    for (int j = 0; j < 4; ++j) MF16(aa[qt][j], ux[hp & 1][a][j], px[hp & 1][a][qt]);
            pin();
            if (hp + 2 < 4) load_pair(hp & 1, hp + 2);
            pin();
        }
        asm volatile("s_nop 7" : "+v"(aa[0][0]), "+v"(aa[0][1]), "+v"(aa[0][2]), "+v"(aa[0][3]), "+v"(aa[1][0]), "+v"(aa[1][1]), "+v"(aa[1][2]), "+v"(aa[1][3]));
        asm volatile("" : "+v"(ab_[0][0]), "+v"(ab_[0][1]), "+v"(ab_[0][2]), "+v"(ab_[0][3]), "+v"(ab_[1][0]), "+v"(ab_[1][1]), "+v"(ab_[1][2]), "+v"(ab_[1][3]), "+v"(az[0]), "+v"(az[1]));
#pragma unroll
        for (int qt = 0; qt < 2; ++qt) {
            const u32 ra = w * 8704 + (16 * qt + cc) * 272 + g * 16;
#pragma unroll
            for (int j = 0; j < 4; ++j) { lds_st128(RA + ra + j * 64, __builtin_bit_cast(u32x4, aa[qt][j])); lds_st128(RB + ra + j * 64, __builtin_bit_cast(u32x4, ab_[qt][j])); }
            if (g == 0) *(LAS float*)(size_t)(RZ + (w * 32 + 16 * qt + cc) * 4) = az[qt][0];
        }
        bar();
        {
            const u32 tid = w * 64 + lane, row = tid >> 3, c = tid & 7;
            f32x4 a0 = {0.f, 0.f, 0.f, 0.f}, a1 = a0, b0 = a0, b1 = a0;
            float z = 0.f;
#pragma unroll
            for (int ww = 0; ww < 4; ++ww) {
                const u32 ra = ww * 8704 + row * 272 + c * 32;
                a0 += __builtin_bit_cast(f32x4, lds128(RA + ra)); a1 += __builtin_bit_cast(f32x4, lds128(RA + ra + 16));
                b0 += __builtin_bit_cast(f32x4, lds128(RB + ra)); b1 += __builtin_bit_cast(f32x4, lds128(RB + ra + 16));
                z += *(const LAS float*)(size_t)(RZ + (ww * 32 + row) * 4);
            }
            const float gt = 1.0f / (1.0f + __expf(-z)), gn = 1.0f - gt;
            const f32x4 o0 = a0 * gt + b0 * gn, o1 = a1 * gt + b1 * gn;
            const u32x4 o = {cvtpk(o0[0], o0[1]), cvtpk(o0[2], o0[3]), cvtpk(o1[0], o1[1]), cvtpk(o1[2], o1[3])};
            *(u32x4*)(p.out + (size_t)(32 * s + row) * 1024 + (64 * kb + 8 * c) * 2) = o;
        }
        bar();
    }
}

__global__ void __launch_bounds__(256, 1) mega(Params p) {
    __shared__ __attribute__((aligned(16))) char smem[163840];
    asm volatile("" :: "v"((u32)(size_t)(LAS char*)smem));
    const u32 tid = threadIdx.x, lane = tid & 63, w = rfl(tid >> 6);
    const u32 b = blockIdx.x, xg = b & 7, k = b >> 3;
    const rsrc_t ws = __builtin_amdgcn_make_buffer_rsrc(p.ws, 0, 256u * MB, 0x00020000);
    STAMP(0);
    if (xg < 4) {
        if (k < 8) role_qk(p, ws, 8 * xg + k, w, lane);
        else if (k < 24) { wvo_unit(p, ws, 16 * xg + (k - 8), w, lane); bar(); role_vo(p, ws, 16 * xg + (k - 8), w, lane); }
        else {
            const u32 t = 8 * xg + (k - 24);
            fill_exch(ws, t, tid); bar();
            b1_unit(p, ws, 64 + t, w, lane); bar();
            if (!(p.dbg & 16)) role_t(p, ws, 2 * xg + ((k - 24) & 1), (k - 24) >> 1, w, lane);
        }
    }
    else if (xg == 4) role_x(p, ws, k, w, lane);
    else if (xg == 5) role_v(p, ws, k, w, lane);
    else {
        const u32 i = (xg - 6) * 32 + k;
        wvo_unit(p, ws, 64 + i, w, lane); bar();
        b1_unit(p, ws, i, w, lane); bar();
        if (!(p.dbg & 32)) role_b2(p, ws, i >> 1, i & 1, w, lane);
    }
    STAMP(4);
}

extern "C" void kernel_launch(void* const* d_in, const int* in_sizes, int n_in, void* d_out, int out_size, void* d_ws, size_t ws_size, hipStream_t stream) {
    Params p;
    p.x = (const char*)d_in[0]; p.mem0 = (const char*)d_in[1]; p.wq = (const char*)d_in[2]; p.wk = (const char*)d_in[3];
    p.wv = (const char*)d_in[4]; p.wo = (const char*)d_in[5]; p.wqkv = (const char*)d_in[8]; p.bwo = (const char*)d_in[9];
    p.gw = (const char*)d_in[10]; p.out = (char*)d_out; p.ws = (char*)d_ws; p.dq = DLYQ0; p.dp = DLYP0; p.dm = DLYM0; p.up = DLY_UP; p.down = DLY_DOWN; p.par = 0; p.dbg = 0; p.dq0 = DLYQ00;
    (void)hipMemsetAsync(d_ws, 0, FLAG_BYTES, stream);
    hipLaunchKernelGGL(mega, dim3(256), dim3(256), 0, stream, p);
}
```
